```python
import torch

def _softmax(x, axis=-1):
    return torch.softmax(x, dim=axis)
def _gelu(x):
    return torch.nn.functional.gelu(x, approximate="tanh")
def _silu(x):
    return torch.nn.functional.silu(x)
def _sigmoid(x):
    return torch.sigmoid(x)
def _layernorm(x, axis=-1, eps=1e-5):
    m = x.mean(dim=axis, keepdim=True)
    v = x.var(dim=axis, keepdim=True, unbiased=False)
    return (x - m) / torch.sqrt(v + eps)
def _scatter_add(tgt, idx, src, axis=0):
    return tgt.clone().index_add_(axis, idx, src)
def _segment_sum(src, seg, n):
    out = torch.zeros((n,) + src.shape[1:], dtype=src.dtype)
    return out.index_add_(0, seg, src)
def _pad(x, pw):
    flat = tuple(v for pair in reversed(pw) for v in pair)
    return torch.nn.functional.pad(x, flat)

kernel_name = 'concat_heads__spectral_fnet__ssm_conv_h3__prompt'

def setup_inputs():

    _w = (lambda a: (lambda t: t.to(torch.bfloat16) if t.is_floating_point() else t)(torch.as_tensor(a)))
    def _A_setup():
        B, S, D = 1, 2048, 640
        ns = torch.arange(S)[:, None] * torch.arange(S)[None, :]
        nd = torch.arange(D)[:, None] * torch.arange(D)[None, :]
        Ws_re = torch.cos(-2.0 * torch.pi * ns / S).to(float)
        Ws_im = torch.sin(-2.0 * torch.pi * ns / S).to(float)
        Wd_re = torch.cos(-2.0 * torch.pi * nd / D).to(float)
        Wd_im = torch.sin(-2.0 * torch.pi * nd / D).to(float)
        return {
            "x": _w((lambda *s: torch.randn(*s, dtype=torch.bfloat16) * 0.05)(B, S, D)),
            "dft_seq_re": _w(Ws_re),
            "dft_seq_im": _w(Ws_im),
            "dft_hid_re": _w(Wd_re),
            "dft_hid_im": _w(Wd_im),
        }
    _A_in = _A_setup()

    def _B_setup():
        B, S, D = 1, 2048, 640
        ks = 4
        return {
            "q": _w((lambda *s: torch.randn(*s, dtype=torch.bfloat16) * 0.05)(B, S, D)),
            "k": _w((lambda *s: torch.randn(*s, dtype=torch.bfloat16) * 3.2)(B, S, D)),
            "v": _w((lambda *s: torch.randn(*s, dtype=torch.bfloat16) * 3.2)(B, S, D)),
            "shift_k": _w((lambda *s: torch.randn(*s, dtype=torch.bfloat16) * 3.2)(D, ks) * 0.5),
            "ssm_k": _w((lambda *s: torch.randn(*s, dtype=torch.bfloat16) * 3.2)(D, S) * (1.0 / S ** 0.5)),
            "ssm_D": _w((lambda *s: torch.randn(*s, dtype=torch.bfloat16) * 3.2)(D)),
        }
    _B_in = _B_setup()

    return {
        "A_x": _A_in["x"],
        "A_dft_seq_re": _A_in["dft_seq_re"],
        "A_dft_seq_im": _A_in["dft_seq_im"],
        "A_dft_hid_re": _A_in["dft_hid_re"],
        "A_dft_hid_im": _A_in["dft_hid_im"],
        "B_q": _B_in["q"],
        "B_k": _B_in["k"],
        "B_v": _B_in["v"],
        "B_shift_k": _B_in["shift_k"],
        "B_ssm_k": _B_in["ssm_k"],
        "B_ssm_D": _B_in["ssm_D"],
    }

def reference(A_x, A_dft_seq_re, A_dft_seq_im, A_dft_hid_re, A_dft_hid_im, B_q, B_k, B_v, B_shift_k, B_ssm_k, B_ssm_D):
    def _A_ref(x, dft_seq_re, dft_seq_im, dft_hid_re, dft_hid_im):
        a = torch.einsum("bsd,ed->bse", x, dft_hid_re)
        b = torch.einsum("bsd,ed->bse", x, dft_hid_im)
        y = torch.einsum("ts,bse->bte", dft_seq_re, a) - torch.einsum("ts,bse->bte", dft_seq_im, b)
        return y

    def _B_ref(q, k, v, shift_k, ssm_k, ssm_D):
        B, S, D = q.shape
        ks = shift_k.shape[-1]
        k_pad = _pad(k, ((0, 0), (ks - 1, 0), (0, 0)))
        k_shift = torch.zeros_like(k)
        for t in range(ks):
            k_shift = k_shift + k_pad[:, t:t + S, :] * shift_k[:, ks - 1 - t][None, None, :]
        kv = k_shift * v
        L = 2 * S
        kv_pad = _pad(kv, ((0, 0), (0, S), (0, 0)))
        kf_pad = _pad(ssm_k, ((0, 0), (0, S)))
        kv_f = (lambda x, axes=(-2,-1): torch.fft.rfft2(x.float(), dim=axes))(kv_pad[:, None, :, :], axes=(1, 2))
        kf_f = (lambda x, axes=(-2,-1): torch.fft.rfft2(x.float(), dim=axes))(kf_pad[:, None, :], axes=(1, 2))
        kf_f = (lambda x, *d: x.permute(*d))(kf_f, 1, 2, 0)
        y_f = kv_f * kf_f[None, :, :, :]
        y = (lambda x, s, axes=(-2,-1): torch.fft.irfft2(x, s=s, dim=axes).to(torch.bfloat16))(y_f, (1, L), axes=(1, 2))[:, 0, :S, :]
        y = y + kv * ssm_D[None, None, :]
        return q * y

    a = _A_ref(A_x, A_dft_seq_re, A_dft_seq_im, A_dft_hid_re, A_dft_hid_im)
    b = _B_ref(B_q, B_k, B_v, B_shift_k, B_ssm_k, B_ssm_D)
    return torch.concatenate([a, b], axis=-1)
```

```cpp
#include <hip/hip_runtime.h>
#include <hip/hip_bf16.h>

typedef unsigned u32; typedef unsigned long long u64;
typedef __attribute__((ext_vector_type(8))) __bf16 bf16x8;
typedef __attribute__((ext_vector_type(2))) __bf16 bf16x2;
typedef __attribute__((ext_vector_type(4))) short s16x4;
typedef __attribute__((ext_vector_type(2))) float f32x2;
typedef __attribute__((ext_vector_type(4))) float f32x4;
typedef __attribute__((ext_vector_type(2))) unsigned u32x2;
typedef __attribute__((ext_vector_type(4))) unsigned u32x4;
#define LAS __attribute__((address_space(3)))
#define GAS __attribute__((address_space(1)))
#define DEVI __device__ __forceinline__
#pragma clang diagnostic ignored "-Wint-to-pointer-cast"

#define BUMP_WAIT 13000
#define PQ_NAP 6
#define CV_SPREAD 11
#define PUREW 6
#define PACE_LEAD 4
#define PACE_STEP 570
#define SDEPTH 5
#define AINIT 3
#define KROT 4
#define SPF_B0 2
#define SPF_R 2
#define W_U 1
#define W_SIG 7
#define W_SPLIT 8
#define PEEK_AT 10
#define W_PRE 10
#define L2_RATE 24
#define PREP_DELAY 300
#define KV_POLL_DELAY 1500
#define CODE_SPAN 29952

constexpr int S = 2048, D = 640, NA = 32;
constexpr size_t WS_EPOCH = 0, WS_FPQ = 4096, WS_FKV = WS_FPQ + 160 * 128;
constexpr size_t WS_PQ = 65536, WS_KVT = WS_PQ + 160 * 32768, WS_END = WS_KVT + 640 * 4096;
constexpr int LDS_BYTES = 163840;

struct Params { const char *x, *cr, *ci, *wr, *wi, *q, *k, *v, *sk, *h, *dd; char *out, *ws; };

#define SB() __builtin_amdgcn_sched_barrier(0)
#define BAR() do{ SB(); __builtin_amdgcn_s_barrier(); asm volatile("" ::: "memory"); SB(); }while(0)
#define WAITV(n) asm volatile("s_waitcnt vmcnt(%0)"::"i"(n):"memory")
#define WAITL(n) do{ SB(); __builtin_amdgcn_s_waitcnt(0xC07F | ((n) << 8)); SB(); }while(0)

#define DMA4(SUF, v0, v1, v2, v3, base, lds) asm volatile( \
  "s_mov_b32 m0, %5\n\ts_nop 0\n\t" \
  "global_load_lds_dwordx4 %0, %4" SUF "\n\t" \
  "global_load_lds_dwordx4 %1, %4 offset:1024" SUF "\n\t" \
  "global_load_lds_dwordx4 %2, %4 offset:2048" SUF "\n\t" \
  "global_load_lds_dwordx4 %3, %4 offset:3072" SUF \
  :: "v"(v0), "v"(v1), "v"(v2), "v"(v3), "s"(base), "s"(lds) : "memory")
#define DMA1(SUF, v0, base, lds) asm volatile("s_mov_b32 m0, %2\n\ts_nop 0\n\tglobal_load_lds_dwordx4 %0, %1" SUF :: "v"(v0), "s"(base), "s"(lds) : "memory")
#define TOUCH(v0, base, lds) asm volatile("s_mov_b32 m0, %2\n\ts_nop 0\n\tglobal_load_lds_dword %0, %1" :: "v"(v0), "s"(base), "s"(lds) : "memory")

DEVI u32x4 lds128u(u32 a) { return *(LAS u32x4*)a; }
DEVI u32x2 lds64u(u32 a) { return *(LAS u32x2*)a; }
DEVI u32 lds32u(u32 a) { return *(LAS u32*)a; }
DEVI u32x2 ldstr(u32 a) { return __builtin_bit_cast(u32x2, __builtin_amdgcn_ds_read_tr16_b64_v4i16((LAS s16x4*)a)); }
DEVI u32 pk(float lo, float hi) { f32x2 v = {lo, hi}; return __builtin_bit_cast(u32, __builtin_convertvector(v, bf16x2)); }
DEVI float blo(u32 x) { return __builtin_bit_cast(float, x << 16); }
DEVI float bhi(u32 x) { return __builtin_bit_cast(float, x & 0xffff0000u); }
template <int OFF> DEVI void st128_dev(u32 vo, char* base, u32x4 v) { asm volatile("global_store_dwordx4 %0, %1, %2 offset:%3 sc1\n\ts_nop 1" :: "v"(vo), "v"(v), "s"(base), "i"(OFF) : "memory"); }
DEVI u64 ld64_dev(const char* a) { return __hip_atomic_load((const GAS u64*)a, __ATOMIC_RELAXED, __HIP_MEMORY_SCOPE_AGENT); }
DEVI void post_line(char* a, u64 v, int lane) { asm volatile("s_mov_b64 exec, 0xffff\n\tglobal_store_dwordx2 %0, %1, %2 sc1\n\ts_mov_b64 exec, -1" :: "v"(lane * 8), "v"(v), "s"(a) : "memory"); }
DEVI u64 read_tag(const Params& p) {
  u64 e;
  asm volatile("s_load_dwordx2 %0, %1, 0x0\n\ts_waitcnt lgkmcnt(0)" : "=&s"(e) : "s"(p.ws + WS_EPOCH) : "memory");
  return e + 1;
}
template <int NAP = 2> DEVI void poll(const char* a, u64 tag) {
#pragma unroll 1
  for (int sp = 0; sp < (1 << 22); ++sp) { if (__all(ld64_dev(a) == tag)) break; __builtin_amdgcn_s_sleep(NAP); }
  __builtin_amdgcn_fence(__ATOMIC_ACQUIRE, "wavefront");
}
DEVI f32x4 mma(u32x4 a, u32x4 b, f32x4 c) { return __builtin_amdgcn_mfma_f32_16x16x32_bf16(__builtin_bit_cast(bf16x8, a), __builtin_bit_cast(bf16x8, b), c, 0, 0, 0); }
DEVI u32x4 cat(u32x2 a, u32x2 b) { return (u32x4){a[0], a[1], b[0], b[1]}; }
DEVI u32 sload(const char* a) { return *(const __attribute__((address_space(4))) u32*)a; }
DEVI u32 cyc() { return (u32)__builtin_amdgcn_s_memtime(); }

#define SPF8(d, base, off) asm volatile( \
  "s_load_dword %0, %1, %2\n\ts_load_dword %0, %1, %2 offset:0x40\n\ts_load_dword %0, %1, %2 offset:0x80\n\ts_load_dword %0, %1, %2 offset:0xc0\n\t" \
  "s_load_dword %0, %1, %2 offset:0x100\n\ts_load_dword %0, %1, %2 offset:0x140\n\ts_load_dword %0, %1, %2 offset:0x180\n\ts_load_dword %0, %1, %2 offset:0x1c0" \
  : "+s"(d) : "s"(base), "s"(off) : "memory")
#define SPF_END(d) asm volatile("s_waitcnt lgkmcnt(0)" : "+s"(d) :: "memory")
DEVI const char* spf_base(const Params& p, int xcd, int w80) {
  const int row = w80 * SPF_R;
  return (row < 256 ? p.cr : p.ci) + (size_t)(xcd * 256 + (row & 255)) * 4096;
}
DEVI u32 spf_off(int n, int rot) { return ((n & 3) * SPF_R >> 2) * 4096 + (((n >> 2) + SPF_B0 + (rot >> 2)) & 7) * 512; }
constexpr int SPF_N = (8 - SPF_B0) * 4;

constexpr u32 A_XOFF = 16384, A_PQ = 98304;
constexpr u32 B_CH = 8320, B_H1 = 4160, B_U = 8 * B_CH, B_UC = 4608, B_DUMMY = 162816, B_HOLD = B_DUMMY + 512, B_GO = B_HOLD + 16, B_SCR = B_DUMMY - 4 * 17408, B_Y = 106496;

DEVI const char* w_ptr(const Params& p, int tn, int u) {
  int sl = tn + (u >> 2); if (sl >= 5) sl -= 5;
  return ((u & 2) ? p.wi : p.wr) + sl * 256 + (u & 1) * 128;
}
DEVI u32 u_addr(int o) { return 128 * (o >> 3) + 64 * ((o & 1) ^ ((o >> 4) & 1)) + 16 * ((o >> 1) & 3); }

struct G1 { f32x4 acc[4][4]; u32x2 fa[2][4][2]; u32x4 fb[2][4]; u32 aB[2][4][2], bB[2][2]; };
template <int I, int BF, int K, int ST> DEVI void g1_rd(G1& r) {
  constexpr int H = ST >> 1, SO = (ST & 1) * 32768;
  if constexpr (I == 0) r.fb[BF][0] = lds128u(r.bB[H][K] + SO);
  else if constexpr (I < 3) r.fa[BF][0][I - 1] = ldstr(r.aB[H][0][I - 1] + SO + 8192 * K);
  else if constexpr (I < 6) r.fb[BF][I - 2] = lds128u(r.bB[H][K] + SO + 2048 * (I - 2));
  else r.fa[BF][(I - 4) >> 1][I & 1] = ldstr(r.aB[H][(I - 4) >> 1][I & 1] + SO + 8192 * K);
  SB();
}
template <int BF, int K, int ST, int M = 0> DEVI void g1_group(G1& r) {
  constexpr int c = M >> 2, t = (c & 1) ? 3 - (M & 3) : (M & 3);
  r.acc[c][t] = mma(cat(r.fa[BF][c][0], r.fa[BF][c][1]), r.fb[BF][t], r.acc[c][t]); SB();
  if constexpr (M < 12) g1_rd<M, BF ^ 1, K, ST>(r);
  if constexpr (M < 15) g1_group<BF, K, ST, M + 1>(r);
}
template <int I = 0> DEVI void g1_first(G1& r) { g1_rd<I, 0, 0, 0>(r); if constexpr (I < 11) g1_first<I + 1>(r); }
template <int ST> DEVI void g1_step(G1& r) {
  WAITL(0); g1_group<0, 1, ST>(r);
  WAITL(0); BAR(); g1_group<1, 0, (ST + 1) & 3>(r);
}
DEVI void g1_init(G1& r, int wid, int lane) {
  const int wd = wid & 1, wr = wid >> 1, g = lane >> 4, q = (lane & 15) >> 2, pp = lane & 3;
#pragma unroll
  for (int c = 0; c < 4; ++c)
#pragma unroll
    for (int t = 0; t < 2; ++t)
    {
      r.aB[0][c][t] = A_XOFF + 256 * (8 * g + 4 * t + q) + 16 * ((8 * wd + 2 * c + (pp >> 1)) ^ ((q << 2) | ((2 * g + t) & 3))) + 8 * (pp & 1);
      r.aB[1][c][t] = r.aB[0][c][t] + 65536;
      asm volatile("" : "+v"(r.aB[0][c][t]), "+v"(r.aB[1][c][t]));
    }
  const int row = 64 * wr + (lane & 15);
#pragma unroll
  for (int k = 0; k < 2; ++k) {
    r.bB[0][k] = 128 * row + 16 * ((4 * k + g) ^ ((row >> 1) & 7));
    r.bB[1][k] = r.bB[0][k] + 65536;
    asm volatile("" : "+v"(r.bB[0][k]), "+v"(r.bB[1][k]));
  }
#pragma unroll
  for (int i = 0; i < 4; ++i)
#pragma unroll
    for (int j = 0; j < 4; ++j) r.acc[i][j] = (f32x4){0.f, 0.f, 0.f, 0.f};
}

constexpr int w_slot(int u) { return u % 6; }
struct G2 { f32x4 y[2][4]; u32x4 gw[2][2][2], gp[2][2][4], pw[4][2][2]; u32 wa[2][2], pa; };
template <int U, int K, int E> DEVI u32x4 g2_w(const G2& r) { constexpr int ws = w_slot(U); return lds128u(r.wa[ws / 3][K] + (ws % 3) * 16384 + E * 512); }
template <int I, int U> DEVI void g2_rd(G2& r) {
  constexpr int k = I / 6, j = I % 6, BF = U & 1;
  if constexpr (j == 0 || j == 5) { if constexpr (U > 3) r.gw[BF][k][j / 5] = g2_w<U, k, j / 5>(r); }
  else r.gp[BF][k][j - 1] = lds128u(r.pa + ((U + 4) & 7) * 8192 + k * 4096 + (j - 1) * 1024);
  SB();
}
template <int U, int LAST, int M = 0> DEVI void g2_step(G2& r) {
  constexpr int k = M >> 3, e = (M >> 2) & 1, t = e ? 3 - (M & 3) : (M & 3), BF = U & 1;
  if constexpr (M == 0) WAITL(U > 3 ? 6 : 4);
  if constexpr (M == 3) { WAITL(0); if constexpr (U > 3 && U < 19) BAR(); }
  if constexpr (U > 3) r.y[e][t] = mma(r.gw[BF][k][e], r.gp[BF][k][t], r.y[e][t]);
  else r.y[e][t] = mma(r.pw[U & 3][k][e], r.gp[BF][k][t], r.y[e][t]);
  SB();
  if constexpr (M >= 3 && M < 15 && U != 3 && U < 19) g2_rd<M - 3, U + 1>(r);
  if constexpr (M < 15) g2_step<U, LAST, M + 1>(r);
  else if constexpr (U < LAST) g2_step<U + 1, LAST>(r);
}
template <int U, int I = 0> DEVI void g2_first(G2& r) { g2_rd<I, U>(r); if constexpr (I < 11) g2_first<U, I + 1>(r); }
template <int U0, int I = 0> DEVI void g2_ahead(G2& r) { r.pw[I >> 2][(I >> 1) & 1][I & 1] = g2_w<U0 + (I >> 2), (I >> 1) & 1, I & 1>(r); SB(); if constexpr (I < 15) g2_ahead<U0, I + 1>(r); }

DEVI void a_compute(const Params& p, int tile, int wid, int lane) {
  const int g = lane >> 4, tb = tile / 5, tn = tile % 5;
  G1 r;
  g1_init(r, wid, lane);
  const u64 tag = read_tag(p);
  BAR();
  g1_first(r);
#pragma unroll 1
  for (int s = 0; s < NA; s += 4) {
    g1_step<0>(r); g1_step<1>(r); g1_step<2>(r); g1_step<3>(r);
  }
  {
    char* o = p.ws + WS_PQ + ((size_t)tile * 4 + wid) * 8192;
    const u32 l = A_PQ + (4 + wid) * 8192 + lane * 16;
    const u32 sg = (wid >> 1) ? 0x80008000u : 0u;
#pragma unroll
    for (int k = 0; k < 2; ++k)
#pragma unroll
      for (int t = 0; t < 4; ++t) {
        const f32x4 u0 = r.acc[2 * k][t], u1 = r.acc[2 * k + 1][t];
        const u32 a0 = pk(u0[0], u0[1]) ^ sg, a1 = pk(u0[2], u0[3]) ^ sg, b0 = pk(u1[0], u1[1]) ^ sg, b1 = pk(u1[2], u1[3]) ^ sg;
        const auto r0 = __builtin_amdgcn_permlane32_swap(a0, b0, false, false), r1 = __builtin_amdgcn_permlane32_swap(a1, b1, false, false);
        const auto q0 = __builtin_amdgcn_permlane16_swap(r0[0], r0[1], false, false), q1 = __builtin_amdgcn_permlane16_swap(r1[0], r1[1], false, false);
        const u32x4 v = {q0[0], q1[0], q0[1], q1[1]};
        if (t == 0) st128_dev<0>(lane * 16 + k * 4096, o, v);
        if (t == 1) st128_dev<1024>(lane * 16 + k * 4096, o, v);
        if (t == 2) st128_dev<2048>(lane * 16 + k * 4096, o, v);
        if (t == 3) st128_dev<3072>(lane * 16 + k * 4096, o, v);
        *(LAS u32x4*)(l + (k * 4 + t) * 1024) = v;
      }
    WAITL(0);
  }
  G2 z;
  const int rr = lane & 15;
#pragma unroll
  for (int k = 0; k < 2; ++k) {
    z.wa[0][k] = 128 * (32 * wid + 8 * (rr >> 2) + (rr & 3)) + 16 * ((4 * k + g) ^ (rr >> 1));
    z.wa[1][k] = z.wa[0][k] + 49152;
    asm volatile("" : "+v"(z.wa[0][k]), "+v"(z.wa[1][k]));
  }
  z.pa = A_PQ + lane * 16;
  asm volatile("" : "+v"(z.pa));
#pragma unroll
  for (int j = 0; j < 4; ++j) { z.y[0][j] = (f32x4){0.f, 0.f, 0.f, 0.f}; z.y[1][j] = (f32x4){0.f, 0.f, 0.f, 0.f}; }
  BAR();
  g2_ahead<0>(z);
  g2_first<0>(z);
  WAITL(8);
  g2_step<0, 0>(z);
  WAITV(0);
  BAR();
  if (wid == 0) post_line(p.ws + WS_FPQ + tile * 128, tag, lane);
  g2_step<1, 3>(z);
  BAR();
  BAR();
  g2_first<4>(z);
  g2_step<4, 19>(z);
  {
    constexpr u32 ST = 32768;
#pragma unroll
    for (int t = 0; t < 4; ++t)
      *(LAS u32x4*)(ST + (t * 16 + rr) * 272 + wid * 64 + g * 16) = (u32x4){pk(z.y[0][t][0], z.y[0][t][1]), pk(z.y[0][t][2], z.y[0][t][3]), pk(z.y[1][t][0], z.y[1][t][1]), pk(z.y[1][t][2], z.y[1][t][3])};
    WAITL(0); BAR();
    char* o = p.out + (size_t)(tb * 64 + wid * 16) * 2560 + tn * 256;
    u32x4 v[4];
#pragma unroll
    for (int t = 0; t < 4; ++t) v[t] = lds128u(ST + (wid * 16 + t * 4 + g) * 272 + rr * 16);
#pragma unroll
    for (int t = 0; t < 4; ++t) st128_dev<0>((u32)((t * 4 + g) * 2560 + rr * 16), o, v[t]);
  }
}

struct L2 { u32 vw0, vw1, v0, lo; int sl[5]; const char* pq; };
#define DMA2(SUF, v0, v1, base, lds) asm volatile("s_mov_b32 m0, %3\n\ts_nop 0\n\tglobal_load_lds_dwordx4 %0, %2" SUF "\n\tglobal_load_lds_dwordx4 %1, %2 offset:1024" SUF \
  :: "v"(v0), "v"(v1), "s"(base), "s"(lds) : "memory")
template <int U, int H> DEVI void l2_w(const Params& p, const L2& l) {
  DMA2("", l.vw0, l.vw1, ((U & 2) ? p.wi : p.wr) + l.sl[U >> 2] * 256 + ((U & 1) * 128 + H * 64 * 1280), l.lo + (w_slot(U) * 16384 + H * 8192));
}
template <int U> DEVI void l2_pq(const L2& l) {
  DMA2(" sc1", l.v0, l.v0, l.pq + l.sl[U >> 2] * 32768 + (U & 3) * 8192, l.lo + (A_PQ + ((U + 4) & 7) * 8192));
}
constexpr int WE = W_PRE;
constexpr int l2_cum(int u) { return u < WE ? u - 3 : WE - 4 + 3 * (u - WE + 1); }
constexpr int l2_iss(int i) {
  if (i < 3) return 0;
  const int uq = i == 3 ? 11 : i + 7, uw = i + 5 > 9 ? i + 5 : 9, um = uq < uw ? uq : uw;
  int n = 4 + (L2_RATE * (i - 3) + 5) / 10, cap = l2_cum(um < 19 ? um : 19);
  return (n < cap && i < 17) ? n : cap;
}
template <int N> DEVI void l2_unit(const Params& p, const L2& l) {
  if constexpr (N < WE - 4) l2_pq<4 + N>(l);
  else if constexpr ((N - WE + 4) % 3 < 2) l2_w<WE + (N - WE + 4) / 3, (N - WE + 4) % 3>(p, l);
  else l2_pq<WE + (N - WE + 4) / 3>(l);
}
template <int U, int E> DEVI void l2_ws(const Params& p, const L2& l) { if constexpr (U < E) { l2_w<U, 0>(p, l); l2_w<U, 1>(p, l); l2_ws<U + 1, E>(p, l); } }
template <int N, int E> DEVI void l2_units(const Params& p, const L2& l) { if constexpr (N < E) { l2_unit<N>(p, l); l2_units<N + 1, E>(p, l); } }
template <int I> DEVI void l2_interval(const Params& p, const L2& l) {
  l2_units<l2_iss(I - 1), l2_iss(I)>(p, l);
  static_assert(l2_iss(I) >= l2_cum(I + 1), "schedule");
  WAITV(2 * (l2_iss(I) - l2_cum(I + 1)));
  BAR();
  if constexpr (I < 18) l2_interval<I + 1>(p, l);
}

DEVI void a_load(const Params& p, int tile, int lw, int lane) {
  const int tb = tile / 5, tn = tile % 5;
  u32 vc[4], vx[4];
#pragma unroll
  for (int j = 0; j < 4; ++j) {
    const int qc = 4 * lw + j, row = 8 * qc + (lane >> 3);
    vc[j] = (u32)((tb * 64 + (row & 63)) * 4096 + 16 * ((lane & 7) ^ ((row >> 1) & 7)) + 4096 - j * 1024);
    vx[j] = (u32)((4 * qc + (lane >> 4)) * 1280 + tn * 256 + 16 * ((lane & 15) ^ (((lane >> 4) << 2) | (qc & 3))) + 4096 - j * 1024);
  }
  const int rot = (tile / 20) * KROT;
  const char* const cb = (lw < 2 ? p.cr : p.ci) - 4096;
  const char* const xb = p.x - 4096;
  int kk = rot;
  u32 so = lw * 4096;
#define P1_ISSUE() do { DMA4("", vc[0], vc[1], vc[2], vc[3], cb + kk * 128, so); DMA4("", vx[0], vx[1], vx[2], vx[3], xb + kk * 81920, so + A_XOFF); \
    so = (so + 32768u) & 131071u; kk = (kk + 1) & 31; } while (0)
  {
    constexpr int LC = (AINIT * 512 + 35) / 36, LX = (AINIT * 640 + 43) / 44;
    static_assert(AINIT <= KROT && LC <= 64 && LX <= 64, "share");
    const int j = (tile % 20) * 4 + lw, jc = (j * 9) / 20, isc = ((j + 1) * 9) / 20 != jc;
    if (isc) {
      const int L = min(jc * LC + lane, AINIT * 512 - 1), r = L / AINIT;
      if (lane < LC) asm volatile("s_mov_b32 m0, %1\n\ts_nop 0\n\tglobal_load_lds_dword %0, off" ::
          "v"((r < 256 ? p.cr : p.ci) + (size_t)((tile / 20) * 256 + (r & 255)) * 4096 + (rot + L % AINIT) * 128), "s"(B_DUMMY) : "memory");
    } else if (lane < LX) TOUCH((u32)(min((j - jc) * LX + lane, AINIT * 640 - 1) * 128), p.x + rot * 81920, B_DUMMY);
  }
  P1_ISSUE();
  const u64 tag = read_tag(p);
  P1_ISSUE();
  u32 sd = 0;
  const char* sb = spf_base(p, tile / 20, (tile % 20) * 4 + lw);
  WAITV(8);
  BAR();
#pragma unroll 1
  for (int s = 0; s < NA - 2; ++s) {
    P1_ISSUE();
    if ((s & (4 / SPF_R - 1)) == 0 && s < SPF_N) SPF8(sd, sb, spf_off(s, rot));
    WAITV(8); BAR();
  }
  SPF_END(sd);
  L2 l;
  {
    const int row = 16 * lw + (lane >> 3);
    l.vw0 = (u32)((tn * 128 + row) * 1280 + 16 * ((lane & 7) ^ (((row >> 1) & 1) | ((row >> 2) & 6))));
    l.vw1 = (l.vw0 ^ 32u) + 8 * 1280 - 1024;
    l.v0 = lane * 16;
    l.lo = lw * 2048;
#pragma unroll
    for (int i = 0; i < 5; ++i) { int sl = tn + i; if (sl >= 5) sl -= 5; l.sl[i] = sl; }
    l.pq = p.ws + WS_PQ + (size_t)tb * 5 * 32768 + lw * 2048;
  }
  l2_ws<0, 2>(p, l);
  WAITV(8); BAR();
  l2_ws<2, 3>(p, l);
  BAR();
  l2_ws<3, 6>(p, l);
  WAITV(8);
  BAR();
  BAR();
  int sl = tn + 1 + (lane >> 4); if (sl >= 5) sl -= 5;
  const char* f = p.ws + WS_FPQ + (tb * 5 + sl) * 128;
  u64 seen = 0;
  l2_ws<6, PEEK_AT>(p, l);
  if (lw == 3) { asm volatile("global_load_dwordx2 %0, %1, off sc1" : "+v"(seen) : "v"(f) : "memory"); }
  l2_ws<PEEK_AT, WE>(p, l);
  if (lw == 3) {
    WAITV(4 * (WE - PEEK_AT));
    asm volatile("" : "+v"(seen));
    if (!__all(seen == tag)) poll<PQ_NAP>(f, tag);
  }
  BAR();
  l2_interval<3>(p, l);
}

DEVI void prep_issue(const Params& p, int cb, int s0, u32 reg, int lane) {
  const u32 vo = (u32)((s0 + (lane >> 3)) * 1280 + cb * 128 + (lane & 7) * 16), st = 8 * 1280 - 1024;
  if (s0 > 0) { if (lane >= 40) DMA1("", vo, p.k - 8 * 1280, reg); }
  else *(LAS u32x4*)(reg + lane * 16) = (u32x4){0, 0, 0, 0};
  DMA4("", vo, vo + st, vo + 2 * st, vo + 3 * st, p.k, reg + 1024);
  DMA4("", vo, vo + st, vo + 2 * st, vo + 3 * st, p.k + 32 * 1280, reg + 5120);
  DMA4("", vo, vo + st, vo + 2 * st, vo + 3 * st, p.v, reg + 9216);
  DMA4("", vo, vo + st, vo + 2 * st, vo + 3 * st, p.v + 32 * 1280, reg + 13312);
}
DEVI void lds_add(u32 a, int v) { asm volatile("s_mov_b64 exec, 1\n\tds_add_u32 %0, %1\n\ts_mov_b64 exec, -1" :: "v"(a), "v"(v) : "memory"); }
DEVI void prep_tile(const Params& p, int cb, int s0, u32 reg, int lane, u32x4 wv) {
  const int cp = lane & 31, hb = lane >> 5;
  const f32x2 w[4] = {{blo(wv[0]), blo(wv[2])}, {bhi(wv[0]), bhi(wv[2])}, {blo(wv[1]), blo(wv[3])}, {bhi(wv[1]), bhi(wv[3])}};
  const u32 ka = reg + (8 + hb * 32) * 128 + cp * 4, va = reg + 9216 + hb * 4096 + cp * 4;
  u32 kk[35], vv[32];
#pragma unroll
  for (int i = 0; i < 35; ++i) kk[i] = lds32u(ka + (i - 3) * 128);
  f32x2 k3 = {blo(kk[0]), bhi(kk[0])}, k2 = {blo(kk[1]), bhi(kk[1])}, k1 = {blo(kk[2]), bhi(kk[2])}, ks[32];
#pragma unroll
  for (int e = 0; e < 32; ++e) {
    const f32x2 k0 = {blo(kk[3 + e]), bhi(kk[3 + e])};
    ks[e] = k3 * w[3] + k2 * w[2] + k1 * w[1] + k0 * w[0];
    asm volatile("" : "+v"(ks[e]));
    k3 = k2; k2 = k1; k1 = k0;
  }
  WAITV(0);
#pragma unroll
  for (int i = 0; i < 32; ++i) vv[i] = lds32u(va + i * 128);
  const int L = s0 >> 6;
#pragma unroll
  for (int oo = 0; oo < 4; ++oo) {
    f32x2 a[8];
#pragma unroll
    for (int e = 0; e < 8; ++e) a[e] = ks[oo * 8 + e] * (f32x2){blo(vv[oo * 8 + e]), bhi(vv[oo * 8 + e])};
    u32x4 o0, o1;
#pragma unroll
    for (int j = 0; j < 4; ++j) { o0[j] = pk(a[7 - 2 * j][0], a[6 - 2 * j][0]); o1[j] = pk(a[7 - 2 * j][1], a[6 - 2 * j][1]); }
    const int o = hb * 4 + oo, pos = 4 * ((o & 1) ^ ((L >> 1) & 1)) + (o >> 1);
    *(LAS u32x4*)(reg + (2 * cp) * 144 + pos * 16) = o0;
    *(LAS u32x4*)(reg + (2 * cp + 1) * 144 + pos * 16) = o1;
  }
  char* g = p.ws + WS_KVT + (size_t)cb * 64 * 4096 + L * 128;
  u32x4 t[8];
#pragma unroll
  for (int i = 0; i < 8; ++i) t[i] = lds128u(reg + (8 * i + (lane >> 3)) * 144 + (lane & 7) * 16);
#pragma unroll
  for (int i = 0; i < 8; ++i) st128_dev<0>((u32)((8 * i + (lane >> 3)) * 4096 + (lane & 7) * 16), g, t[i]);
}

struct CV { f32x4 acc[8]; u32x2 car[8], n1[8], n2[8]; u32x4 fa[2][8], fb[2][8]; u32 ha, ut[8], sh; };
constexpr int cv_i(int m) { int i = 0; while (m >= 8 - i) { m -= 8 - i; ++i; } return i; }
constexpr int cv_j(int m) { int i = 0; while (m >= 8 - i) { m -= 8 - i; ++i; } return m; }
DEVI u32 align(u32 hi, u32 lo, u32 sh) { u32 r; asm volatile("v_alignbit_b32 %0, %1, %2, %3" : "=v"(r) : "v"(hi), "v"(lo), "v"(sh)); return r; }
template <int A, int BF> DEVI void cv_align(CV& c) {
  constexpr int i = A >> 2, k = A & 3;
  if constexpr (k == 0) c.fa[BF][i][0] = align(c.car[i][1], c.car[i][0], c.sh);
  if constexpr (k == 1) c.fa[BF][i][1] = align(c.n1[i][0], c.car[i][1], c.sh);
  if constexpr (k == 2) c.fa[BF][i][2] = align(c.n1[i][1], c.n1[i][0], c.sh);
  if constexpr (k == 3) { c.fa[BF][i][3] = align(c.n2[i][0], c.n1[i][1], c.sh); c.car[i] = c.n2[i]; }
  SB();
}
template <int SG, int M = 0> DEVI void cv_step(CV& c) {
  constexpr int i = cv_i(M), j = cv_j(M), NB = SG & 1, HO = SG == 8 ? 4 * B_CH : SG * 16, UO = SG == 8 ? 4 * B_UC : 0;
  constexpr bool fresh = (SG & 7) == 0;
  if constexpr (M == 0) WAITL(0);
  if constexpr (SG > 0) { asm volatile("v_mfma_f32_16x16x32_bf16 %0, %1, %2, %0" : "+v"(c.acc[i + j]) : "v"(c.fa[NB ^ 1][i]), "v"(c.fb[NB ^ 1][j])); SB(); }
  if constexpr (fresh && M < 16 && !(M & 1)) { c.car[M >> 1] = lds64u(c.ha + (M >> 1) * 512 + HO); SB(); }
  if constexpr (M < 16) { if constexpr (M & 1) c.n2[M >> 1] = lds64u(c.ha + (M >> 1) * 512 + HO + 16); else c.n1[M >> 1] = lds64u(c.ha + (M >> 1) * 512 + HO + 8); SB(); }
  else if constexpr (M < 24) { c.fb[NB][M - 16] = lds128u(c.ut[SG & 7] + (M - 16) * 512 + UO); SB(); }
  if constexpr (fresh) {
    if constexpr (M == 8) WAITL(fresh ? 8 : 5);
    if constexpr (M == 16) WAITL(fresh ? 7 : 5);
    if constexpr (M == 24) WAITL(8);
    if constexpr (M >= 8 && M < 24) cv_align<M - 8, NB>(c);
    if constexpr (M >= 24 && M < 32) { cv_align<2 * M - 32, NB>(c); cv_align<2 * M - 31, NB>(c); }
  } else {
    constexpr int A0 = CV_SPREAD;
    if constexpr (M < 8) cv_align<4 * M, NB>(c);
    if constexpr (M == A0) WAITL(A0 + 1 - 6);
    if constexpr (M == A0 + 9) WAITL(A0 + 10 - 16);
    if constexpr (M >= A0 && M < A0 + 24) cv_align<4 * ((M - A0) / 3) + 1 + (M - A0) % 3, NB>(c);
  }
  if constexpr (M < 35) cv_step<SG, M + 1>(c);
}
DEVI void wait_lds(u32 a, u32 n);
DEVI void conv_channels(int wid, int lane, float dd0, float dd1) {
  const int b = lane & 15, g = lane >> 4, w0 = 32 * g + ((b + 1) >> 1);
  CV c;
  c.sh = ((b + 1) & 1) * 16;
  c.ha = wid * B_CH + (w0 & 1) * B_H1 + (w0 & ~1) * 4;
#pragma unroll
  for (int i = 0; i < 8; ++i) c.ut[i] = B_U + wid * B_UC + u_addr(2 * b + 33 - 8 * g - i);
  u32 ue = B_U + wid * B_UC + u_addr(2 * b + (g >> 1) + 32) + 8 * (1 - (g & 1)), ya = wid * B_CH + b * 32 + g * 8;
  cv_step<0>(c);
#pragma unroll 1
  for (int ch = 0; ch < 2; ++ch) {
#pragma unroll
    for (int j = 0; j < 8; ++j) { c.acc[j] = (f32x4){0.f, 0.f, 0.f, 0.f}; asm volatile("" : "+v"(c.ut[j])); }
    asm volatile("s_nop 1" : "+v"(c.acc[0]), "+v"(c.acc[1]), "+v"(c.acc[2]), "+v"(c.acc[3]), "+v"(c.acc[4]), "+v"(c.acc[5]), "+v"(c.acc[6]), "+v"(c.acc[7]), "+v"(c.ha));
    cv_step<1>(c); cv_step<2>(c); cv_step<3>(c); cv_step<4>(c); cv_step<5>(c); cv_step<6>(c); cv_step<7>(c);
    wait_lds(B_HOLD + wid * 4, 1);
    cv_step<8>(c);
    u32x2 uv[8];
#pragma unroll
    for (int j = 0; j < 8; ++j) uv[j] = lds64u(ue + j * 512);
    asm volatile("s_nop 7" : "+v"(c.acc[0]), "+v"(c.acc[1]), "+v"(c.acc[2]), "+v"(c.acc[3]), "+v"(c.acc[4]), "+v"(c.acc[5]), "+v"(c.acc[6]), "+v"(c.acc[7]));
    const float dd = ch ? dd1 : dd0;
#pragma unroll
    for (int j = 0; j < 8; ++j) {
      u32x2 o;
      o[0] = pk(c.acc[j][0] + bhi(uv[j][1]) * dd, c.acc[j][1] + blo(uv[j][1]) * dd);
      o[1] = pk(c.acc[j][2] + bhi(uv[j][0]) * dd, c.acc[j][3] + blo(uv[j][0]) * dd);
      *(LAS u32x2*)(ya + j * 512) = o;
      c.ut[j] += 4 * B_UC;
    }
    ue += 4 * B_UC; ya += 4 * B_CH; c.ha += 4 * B_CH;
  }
}

DEVI void load_h(const Params& p, int ch, int slot, u32 v0) {
  const __amdgpu_buffer_rsrc_t rs = __builtin_amdgcn_make_buffer_rsrc((void*)p.h, 0, D * S * 2, 0x00020000);
#pragma unroll
  for (int cp = 0; cp < 2; ++cp)
    asm volatile("s_mov_b32 m0, %3\n\ts_nop 0\n\t"
                 "buffer_load_dwordx4 %0, %1, %2 offen lds\n\t"
                 "buffer_load_dwordx4 %0, %1, %2 offen offset:1024 lds\n\t"
                 "buffer_load_dwordx4 %0, %1, %2 offen offset:2048 lds\n\t"
                 "buffer_load_dwordx4 %0, %1, %2 offen offset:3072 lds"
                 :: "v"(v0), "s"(rs), "s"(ch * 4096 + cp * 4), "s"(slot * B_CH + cp * B_H1 + 32) : "memory");
}
DEVI void wait_lds(u32 a, u32 n) {
#pragma unroll 1
  for (int sp = 0; sp < (1 << 24); ++sp) { if (__builtin_amdgcn_readfirstlane(*(volatile LAS u32*)a) >= n) break; __builtin_amdgcn_s_sleep(1); }
  asm volatile("" ::: "memory");
}
DEVI void b_compute(const Params& p, int g8, int wid, int lane) {
  const u32 scr = B_SCR + wid * 17408, v0 = lane * 16;
  const int ch0 = g8 * 8 + wid * 2, cb = g8 >> 3, s0 = (g8 & 7) * 256 + wid * 64;
  u32x4 wv;
  asm volatile("global_load_dwordx4 %0, %1, %2" : "=v"(wv) : "v"((u32)((cb * 64 + 2 * (lane & 31)) * 8)), "s"(p.sk) : "memory");
  const u64 tag = read_tag(p);
  *(volatile LAS u32*)(B_HOLD + (lane & 7) * 4) = 0; WAITL(0); BAR();
  if (PREP_DELAY) { const u32 t0 = cyc(); while (cyc() - t0 < PREP_DELAY) __builtin_amdgcn_s_sleep(4); }
  __builtin_amdgcn_s_setprio(2);
  prep_issue(p, cb, s0, scr, lane);
  {
    const u32 ha = sload(p.h + ch0 * 4096), hb = sload(p.h + ch0 * 4096 + 4096);
    if ((lane & 31) < 16) *(LAS u32*)((wid * 2 + (lane >> 5)) * B_CH + ((lane >> 3) & 1) * B_H1 + (lane & 7) * 4) = lane == 15 ? ha : lane == 47 ? hb : 0u;
  }
  WAITV(8);
  asm volatile("" : "+v"(wv));
  prep_tile(p, cb, s0, scr, lane, wv);
  WAITV(0);
  {
    u32 old;
    asm volatile("s_mov_b64 exec, 1\n\tds_add_rtn_u32 %0, %1, %2\n\ts_mov_b64 exec, -1\n\ts_waitcnt lgkmcnt(0)" : "=&v"(old) : "v"(B_GO + 4), "v"(1) : "memory");
    if (__builtin_amdgcn_readfirstlane(old) == 3) post_line(p.ws + WS_FKV + g8 * 128, tag, lane);
  }
  load_h(p, g8 * 8 + wid, wid, v0);
  if (wid == 0) {
    if (KV_POLL_DELAY) { const u32 t0 = cyc(); while (cyc() - t0 < KV_POLL_DELAY) __builtin_amdgcn_s_sleep(4); }
    poll<12>(p.ws + WS_FKV + ((g8 & ~7) + (lane >> 3)) * 128, tag);
    *(volatile LAS u32*)B_GO = 1;
  }
}
DEVI void load_u(const Params& p, int g8, int wid, int lane) {
  wait_lds(B_GO, 1);
  const u32 ur = B_U + wid * B_UC, v0 = lane * 16;
  DMA4(" sc1", v0, v0, v0, v0, p.ws + WS_KVT + (size_t)(g8 * 8 + wid) * 4096, ur + 512);
  if (lane < 32) *(LAS u32x4*)(ur + lane * 16) = (u32x4){0, 0, 0, 0};
}

DEVI void b_final(const Params& p, int g8, int wid, int lane) {
  const u32 a = ((lane & 15) >> 2) * B_CH + (wid * 256 + (lane & 48) + 4 * (lane & 3)) * 2;
  u32x4 y[4], q[4];
#pragma unroll
  for (int it = 0; it < 4; ++it) { y[it] = cat(ldstr(a + it * 128), ldstr(a + it * 128 + 4 * B_CH)); q[it] = lds128u(B_Y + (wid * 256 + it * 64 + lane) * 16); }
#pragma unroll
  for (int it = 0; it < 4; ++it)
    *(GAS u32x4*)(p.out + (size_t)(wid * 256 + it * 64 + lane) * 2560 + 1280 + g8 * 16) =
           (u32x4){pk(blo(y[it][0]) * blo(q[it][0]), bhi(y[it][0]) * bhi(q[it][0])), pk(blo(y[it][1]) * blo(q[it][1]), bhi(y[it][1]) * bhi(q[it][1])),
                   pk(blo(y[it][2]) * blo(q[it][2]), bhi(y[it][2]) * bhi(q[it][2])), pk(blo(y[it][3]) * blo(q[it][3]), bhi(y[it][3]) * bhi(q[it][3]))};
}

constexpr int NSW = 40 + 2 * PUREW;
DEVI void b_stream(const Params& p, int xcd, int sw, int lane) {
  const u32 t0 = cyc();
  constexpr int NC = SPF_B0 * 4, Q0 = SPF_R * 5;
  u32 vC = lane * 4096, vX = lane * 128, vW = lane * 1280;
  asm volatile("" : "+v"(vC), "+v"(vX), "+v"(vW));
  if (lane < 16) {
    int q = sw;
#pragma unroll 1
    for (int ks = AINIT; ks < 32; ++ks) {
      const int c0 = ks < NC ? 0 : Q0, nj = 72 - c0, lim = (ks - PACE_LEAD) * PACE_STEP;
      if (q < nj) { while ((int)(cyc() - t0) < lim) __builtin_amdgcn_s_sleep(2); }
#pragma unroll 1
      for (; q < nj; q += NSW) {
        const int j = q + c0, kr = (ks + xcd * KROT) & 31;
        if (j < 32) TOUCH(vC, ((j & 16) ? p.ci : p.cr) + ((xcd * 16 + (j & 15)) * 65536 + kr * 128), B_DUMMY);
        else TOUCH(vX, p.x + (kr * 40 + j - 32) * 2048, B_DUMMY);
        WAITV(SDEPTH);
      }
      q -= nj;
      if (ks == AINIT + 1 && sw < 40) BAR();
    }
  }
  if (lane < 16) {
#pragma unroll 1
    for (int j = sw; j < 100; j += NSW) { const int i = xcd * 5 + j % 5; TOUCH(vW, w_ptr(p, i >> 3, j / 5) + i * 20480, B_DUMMY); WAITV(SDEPTH); }
  }
}
DEVI void b_stream2(const Params& p, int xcd, int s4, int lane, int u0, int u1, bool q, int nw) {
  u32 vW = lane * 1280;
  asm volatile("" : "+v"(vW));
  if (lane < 16) {
#pragma unroll 1
    for (int j = u0 * 35 + s4; j < u1 * 35; j += nw) {
      const int i0 = j % 35, i = i0 >= xcd * 5 ? i0 + 5 : i0;
      TOUCH(vW, w_ptr(p, i >> 3, j / 35) + i * 20480, B_DUMMY); WAITV(SDEPTH);
    }
    if (q) {
#pragma unroll 1
      for (int j = s4; j < 256; j += nw) { TOUCH(vW, p.q + ((j >> 1) * 20480 + ((xcd * 160) & ~127) + (j & 1) * 128), B_DUMMY); WAITV(SDEPTH); }
    }
  }
}

extern "C" __global__ __launch_bounds__(512, 2) void fnet_h3_kernel(Params p) {
  const int wid = __builtin_amdgcn_readfirstlane(threadIdx.x >> 6), lane = threadIdx.x & 63;
  const int xcd = blockIdx.x & 7, idx = ((blockIdx.x >> 3) + 20) & 31;
  u64 pc; u32 back;
  asm volatile("s_getpc_b64 %0\n.Lfnet_pc:\n\ts_mov_b32 %1, .Lfnet_pc-fnet_h3_kernel" : "=s"(pc), "=s"(back));
  const char* code = (const char*)(pc - back);
  if ((blockIdx.x >> 3) < 4 && wid == 0) TOUCH(min((u32)((blockIdx.x >> 3) * 8192 + lane * 128), (u32)(CODE_SPAN - 128)), code, B_DUMMY);
  const int si = idx - 20, g8 = xcd * 10 + si;
  if (__builtin_expect(idx < 20, 1)) {
    if (__builtin_expect(wid >= 4, 1)) a_load(p, xcd * 20 + idx, wid - 4, lane);
    else a_compute(p, xcd * 20 + idx, wid, lane);
  } else if (__builtin_expect(wid >= 4 || (si >= 10 && wid < PUREW - 4), 1)) {
    b_stream(p, xcd, si < 10 ? si * 4 + wid - 4 : 40 + (si - 10) * PUREW + ((wid + 4) & 7), lane);
    if (wid >= 4) {
      if (si < 10) {
        load_h(p, g8 * 8 + wid, wid, lane * 16);
        b_stream2(p, xcd, si * 4 + wid - 4, lane, 0, W_U, false, 40);
        load_u(p, g8, wid, lane);
        b_stream2(p, xcd, si * 4 + wid - 4, lane, W_U, W_SIG, false, 40);
        static_assert((W_SIG - W_U) * 35 / 40 >= SDEPTH, "those operands are older than the requests still in flight");
        WAITL(0); lds_add(B_HOLD + (wid - 4) * 4, 1);
        b_stream2(p, xcd, si * 4 + wid - 4, lane, W_SIG, W_SPLIT, true, 40);
      } else b_stream2(p, xcd, si * 4 + wid - 44, lane, W_SPLIT, 20, false, 8);
      if (si < 10) {
#pragma unroll 1
        for (int i = 0; i < 8; ++i) { DMA1("", (u32)((((wid - 4) * 8 + i) * 64 + lane) * 1280 + g8 * 16), p.q, B_Y + ((wid - 4) * 8 + i) * 1024); WAITV(1); }
        WAITV(0);
      }
    }
    } else if (si < 10) {
      b_compute(p, g8, wid, lane);
      const u32 d0 = sload(p.dd + g8 * 16 + (wid >> 1) * 4), d1 = sload(p.dd + g8 * 16 + 8 + (wid >> 1) * 4);
      load_u(p, g8, wid, lane);
      __builtin_amdgcn_s_setprio(0);
      WAITV(0);
      conv_channels(wid, lane, (wid & 1) ? bhi(d0) : blo(d0), (wid & 1) ? bhi(d1) : blo(d1));
      WAITL(0);
    } else if (wid >= 2) {
      {
        const u32 t0 = cyc();
        while (cyc() - t0 < 4000) __builtin_amdgcn_s_sleep(8);
        const int w = (si - 10) * 2 + (wid & 1);
        const char* base = w == 0 ? p.ws : w == 1 ? p.out : w == 2 ? p.q : p.h;
        u32 d, off = 0, size = w == 0 ? (u32)WS_END : w == 1 ? S * D * 4 : S * D * 2;
        asm volatile("1:\n\ts_load_dword %0, %2, %1\n\ts_add_u32 %1, %1, %4\n\ts_cmp_lt_u32 %1, %3\n\ts_cbranch_scc1 1b\n\ts_waitcnt lgkmcnt(0)"
                     : "=&s"(d), "+s"(off) : "s"(base), "s"(size), "s"(1 << 20) : "memory", "scc");
        while (cyc() - t0 < 15000) __builtin_amdgcn_s_sleep(8);
        TOUCH(min((u32)(w * 8192 + lane * 128), (u32)(CODE_SPAN - 128)), code, B_DUMMY + 256);
      }
      if (idx == 31 && xcd == 7 && wid == 3) {
      const u64 tag = read_tag(p);
      const char* f = p.ws + WS_FPQ + min(lane, 59) * 128;
      { const u32 t1 = cyc(); while (cyc() - t1 < BUMP_WAIT) __builtin_amdgcn_s_sleep(8); }
#pragma unroll 1
      for (int sp = 0; sp < (1 << 20); ++sp) {
        const u64 f0 = ld64_dev(f), f1 = ld64_dev(f + 60 * 128), f2 = ld64_dev(f + 120 * 128), f3 = ld64_dev(f + 180 * 128);
        if (__all(f0 == tag && f1 == tag && f2 == tag && f3 == tag)) break;
        __builtin_amdgcn_s_sleep(16);
      }
      post_line(p.ws + WS_EPOCH, tag, lane);
      }
    }
  if (si >= 0 && si < 10) { BAR(); b_final(p, g8, wid, lane); }
}

extern "C" void kernel_launch(void* const* d_in, const int* in_sizes, int n_in, void* d_out, int out_size, void* d_ws, size_t ws_size, hipStream_t stream) {
  Params p;
  p.x = (const char*)d_in[0]; p.cr = (const char*)d_in[1]; p.ci = (const char*)d_in[2]; p.wr = (const char*)d_in[3]; p.wi = (const char*)d_in[4];
  p.q = (const char*)d_in[5]; p.k = (const char*)d_in[6]; p.v = (const char*)d_in[7]; p.sk = (const char*)d_in[8]; p.h = (const char*)d_in[9]; p.dd = (const char*)d_in[10];
  p.out = (char*)d_out; p.ws = (char*)d_ws;
  (void)hipFuncSetAttribute((const void*)fnet_h3_kernel, hipFuncAttributeMaxDynamicSharedMemorySize, LDS_BYTES);
  fnet_h3_kernel<<<256, 512, LDS_BYTES, stream>>>(p);
}
```
